```python
import torch

def _softmax(x, axis=-1):
    return torch.softmax(x, dim=axis)
def _gelu(x):
    return torch.nn.functional.gelu(x, approximate="tanh")
def _silu(x):
    return torch.nn.functional.silu(x)
def _sigmoid(x):
    return torch.sigmoid(x)
def _layernorm(x, axis=-1, eps=1e-5):
    m = x.mean(dim=axis, keepdim=True)
    v = x.var(dim=axis, keepdim=True, unbiased=False)
    return (x - m) / torch.sqrt(v + eps)
def _scatter_add(tgt, idx, src, axis=0):
    return tgt.clone().index_add_(axis, idx, src)
def _segment_sum(src, seg, n):
    out = torch.zeros((n,) + src.shape[1:], dtype=src.dtype)
    return out.index_add_(0, seg, src)
def _pad(x, pw):
    flat = tuple(v for pair in reversed(pw) for v in pair)
    return torch.nn.functional.pad(x, flat)

kernel_name = 'standalone__batchnorm__prompt'

def setup_inputs():

    _w = (lambda a: (lambda t: t.to(torch.bfloat16) if t.is_floating_point() else t)(torch.as_tensor(a)))
    B, S, C = 2, 2048, 576
    K = 3
    return {
        "x": _w((lambda *s: torch.randn(*s, dtype=torch.bfloat16) * 0.05)(B, S, C)),
        "w": _w((lambda *s: torch.randn(*s, dtype=torch.bfloat16) * 0.05)(K, C, C) * (1.0 / (K * C) ** 0.5)),
        "mean": _w((lambda *s: torch.randn(*s, dtype=torch.bfloat16) * 0.05)(C) * 0.1),
        "var": _w(torch.abs((lambda *s: torch.randn(*s, dtype=torch.bfloat16) * 0.05)(C)) + 0.5),
        "g": _w((lambda *s: torch.randn(*s, dtype=torch.bfloat16) * 0.05)(C) + 1.0),
        "b": _w((lambda *s: torch.randn(*s, dtype=torch.bfloat16) * 0.05)(C) * 0.1),
    }

def reference(x, w, mean, var, g, b):
    K = 3
    P = K // 2
    S = x.shape[1]
    xp = _pad(x, ((0, 0), (P, P), (0, 0)))
    y = sum(torch.einsum('bsc,cd->bsd', xp[:, k:k + S, :], w[k]) for k in range(K))
    inv_std = 1.0 / torch.sqrt(var + 1e-5)
    y = (y - mean) * (g * inv_std) + b
    return _silu(y)
```

```cpp
#include <hip/hip_runtime.h>
#include <hip/hip_bf16.h>

__device__ __forceinline__ float bf2f(__hip_bfloat16 v){ return __bfloat162float(v); }
__device__ __forceinline__ float rbf(float v){ return __bfloat162float(__float2bfloat16(v)); }

__global__ void naive_kernel(const __hip_bfloat16* x, const __hip_bfloat16* w,
                             const __hip_bfloat16* mean, const __hip_bfloat16* var,
                             const __hip_bfloat16* g, const __hip_bfloat16* b,
                             __hip_bfloat16* out, int B, int S, int C) {
  int idx = blockIdx.x * blockDim.x + threadIdx.x;
  if (idx >= B * S * C) return;
  int d = idx % C;
  int s = (idx / C) % S;
  int bb = idx / (C * S);
  float acc = 0.f;
  for (int k = 0; k < 3; ++k) {
    int sr = s + k - 1;
    if (sr < 0 || sr >= S) continue;
    const __hip_bfloat16* xr = x + ((long)bb * S + sr) * C;
    const __hip_bfloat16* wk = w + (long)k * C * C + d;
    for (int c = 0; c < C; ++c) acc += bf2f(xr[c]) * bf2f(wk[(long)c * C]);
  }
  float inv_std = rbf(1.0f / rbf(sqrtf(rbf(bf2f(var[d]) + 1e-5f))));
  float sc = rbf(bf2f(g[d]) * inv_std);
  float y = (acc - bf2f(mean[d])) * sc + bf2f(b[d]);
  float o = y / (1.0f + __expf(-y));
  out[idx] = __float2bfloat16(o);
}

extern "C" void kernel_launch(void* const* d_in, const int* in_sizes, int n_in,
                              void* d_out, int out_size, void* d_ws, size_t ws_size,
                              hipStream_t stream) {
  const __hip_bfloat16* x = (const __hip_bfloat16*)d_in[0];
  const __hip_bfloat16* w = (const __hip_bfloat16*)d_in[1];
  const __hip_bfloat16* mean = (const __hip_bfloat16*)d_in[2];
  const __hip_bfloat16* var = (const __hip_bfloat16*)d_in[3];
  const __hip_bfloat16* g = (const __hip_bfloat16*)d_in[4];
  const __hip_bfloat16* b = (const __hip_bfloat16*)d_in[5];
  __hip_bfloat16* out = (__hip_bfloat16*)d_out;
  int B = 2, S = 2048, C = 576;
  int n = B * S * C;
  naive_kernel<<<(n + 255) / 256, 256, 0, stream>>>(x, w, mean, var, g, b, out, B, S, C);
}
```

```cpp
#include <hip/hip_runtime.h>
#include <hip/hip_bf16.h>
#include <stdint.h>

typedef short    bf16x8 __attribute__((ext_vector_type(8)));
typedef short    bf16x4 __attribute__((ext_vector_type(4)));
typedef float    f32x4  __attribute__((ext_vector_type(4)));
typedef float    f32x2  __attribute__((ext_vector_type(2)));
typedef __bf16   hbf16x2 __attribute__((ext_vector_type(2)));
typedef unsigned u32x4  __attribute__((ext_vector_type(4)));
typedef unsigned u32x2  __attribute__((ext_vector_type(2)));

#define LDS_AS __attribute__((address_space(3)))

namespace {
constexpr int kS = 2048, kC = 576, kB = 2;
constexpr int kBM = 158;
constexpr int kRows = 160;
constexpr int kMTB = 13;
constexpr int kNT = 9;
constexpr int kTiles = kB * kMTB * kNT;
constexpr int kXStage = kRows * 128;
constexpr int kWStage = 3 * 64 * 128;
constexpr int kWBase = 3 * kXStage;
constexpr int kLds = kWBase + 3 * kWStage;
constexpr int kTWave = 32 * 160;
constexpr int kTBase = kWBase;
constexpr int kExch = kTBase + 4 * kTWave;
constexpr unsigned kOOB = 0x80000000u;
static_assert(kExch + 1024 <= kWBase + kWStage, "epilogue scratch must stay inside w stage 0");
}

__device__ __forceinline__ float bf16_bits_to_f32(unsigned short v) { return __uint_as_float((unsigned)v << 16); }
__device__ __forceinline__ float round_bf16(float v) { return __bfloat162float(__float2bfloat16(v)); }
__device__ __forceinline__ unsigned pack_bf16x2(float lo, float hi) {
  f32x2 v = {lo, hi};
  return __builtin_bit_cast(unsigned, __builtin_convertvector(v, hbf16x2));
}
__device__ __forceinline__ float silu_f32(float t) {
  const float e = __builtin_amdgcn_exp2f(t * -1.4426950408889634f);
  return t * __builtin_amdgcn_rcpf(1.0f + e);
}

__global__ __launch_bounds__(512, 2)
void conv3_bn_silu_kernel(const __hip_bfloat16* __restrict__ x, const __hip_bfloat16* __restrict__ w,
                          const __hip_bfloat16* __restrict__ mean, const __hip_bfloat16* __restrict__ var,
                          const __hip_bfloat16* __restrict__ gam, const __hip_bfloat16* __restrict__ bet,
                          __hip_bfloat16* __restrict__ out) {
  extern __shared__ __attribute__((aligned(16))) char smem[];
  const int tid = threadIdx.x;
  const int lane = tid & 63;
  const int wave = __builtin_amdgcn_readfirstlane(tid >> 6);

  const int bid = blockIdx.x;
  const int grp8 = bid & 7, idx = bid >> 3;
  const int t = (grp8 < 2 ? grp8 * 30 : 60 + (grp8 - 2) * 29) + idx;
  const int mt = t / kNT, nt = t - mt * kNT;
  const int batch = mt / kMTB, mb = mt - batch * kMTB;
  const int s0 = mb * kBM;
  const int n0 = nt * 64;

  if (wave >= 4) {
    const int lw = wave - 4;
    const auto rx = __builtin_amdgcn_make_buffer_rsrc((void*)x, (short)0, kB * kS * kC * 2, 0x00020000);
    const auto rw = __builtin_amdgcn_make_buffer_rsrc((void*)w, (short)0, 3 * kC * kC * 2, 0x00020000);
    const int klo = lane >> 3, cp = lane & 7;
    unsigned xoff[5];
#pragma unroll
    for (int jj = 0; jj < 5; ++jj) {
      const int L = 8 * (5 * lw + jj) + klo;
      const int Lm = L >= 80 ? L - 80 : L;
      const int sw = ((Lm / 20) << 1) | ((L >> 1) & 1);
      const int c = cp ^ sw;
      const int s = s0 - 1 + L;
      xoff[jj] = ((unsigned)s < (unsigned)kS) ? (unsigned)(((batch * kS + s) * kC + c * 8) * 2) : kOOB;
    }
    unsigned woff[2];
#pragma unroll
    for (int par = 0; par < 2; ++par) {
      const int fw = (klo & 2) | (par << 2);
      woff[par] = (unsigned)((klo * kC + n0 + (cp ^ fw) * 8) * 2);
    }
    auto issue = [&](int cc, int st) {
#pragma unroll
      for (int jj = 0; jj < 5; ++jj)
        __builtin_amdgcn_raw_ptr_buffer_load_lds(rx, (LDS_AS void*)(smem + st * kXStage + (5 * lw + jj) * 1024), 16,
                                                 xoff[jj], cc * 128, 0, 0);
#pragma unroll
      for (int ii = 0; ii < 6; ++ii) {
        const int u = 6 * lw + ii;
        const int tap = u >> 3, rg = u & 7;
        __builtin_amdgcn_raw_ptr_buffer_load_lds(rw, (LDS_AS void*)(smem + kWBase + st * kWStage + u * 1024), 16,
                                                 woff[ii & 1], (tap * kC + rg * 8 + cc * 64) * (kC * 2), 0, 0);
      }
    };
    issue(0, 0);
    issue(1, 1);
    issue(2, 2);
    asm volatile("s_waitcnt vmcnt(22)" ::: "memory");
    __builtin_amdgcn_s_barrier();
#pragma unroll
    for (int c = 0; c < 8; ++c) {
      if (c < 7) asm volatile("s_waitcnt vmcnt(11)" ::: "memory");
      else       asm volatile("s_waitcnt vmcnt(0)" ::: "memory");
      __builtin_amdgcn_s_barrier();
      if (c + 3 <= 8) issue(c + 3, c % 3);
    }
    return;
  }

  const int wm = wave >> 1, wn = wave & 1;
  const int g = lane >> 4, l15 = lane & 15, q = l15 >> 2, p = lane & 3;

  const int ch0 = n0 + 32 * wn + l15;
  unsigned short pm[2], pv[2], pg[2], pb[2];
#pragma unroll
  for (int jc = 0; jc < 2; ++jc) {
    pm[jc] = ((const unsigned short*)mean)[ch0 + 16 * jc];
    pv[jc] = ((const unsigned short*)var)[ch0 + 16 * jc];
    pg[jc] = ((const unsigned short*)gam)[ch0 + 16 * jc];
    pb[jc] = ((const unsigned short*)bet)[ch0 + 16 * jc];
  }

  const int swA = (q << 1) | (p >> 1);
  int aoff[2];
#pragma unroll
  for (int ks = 0; ks < 2; ++ks) aoff[ks] = (80 * wm + 20 * q + p) * 128 + (((4 * ks + g) ^ swA) << 4);
  const int fw = (q & 2) | ((g & 1) << 2);
  int boff[2];
#pragma unroll
  for (int jc = 0; jc < 2; ++jc)
    boff[jc] = kWBase + (8 * g + q) * 128 + (((4 * wn + 2 * jc + (p >> 1)) ^ fw) << 4) + (p & 1) * 8;

  f32x4 acc[5][3][2];
#pragma unroll
  for (int i = 0; i < 5; ++i)
#pragma unroll
    for (int tp = 0; tp < 3; ++tp)
#pragma unroll
      for (int jc = 0; jc < 2; ++jc) acc[i][tp][jc] = f32x4{0.f, 0.f, 0.f, 0.f};

  bf16x8 aF[2][5], bF[2][3][2];
  auto read_frags = [&](int buf, int st, int ks) {
#pragma unroll
    for (int i = 0; i < 5; ++i) aF[buf][i] = *(const bf16x8*)(smem + aoff[ks] + st * kXStage + i * 512);
#pragma unroll
    for (int tp = 0; tp < 3; ++tp)
#pragma unroll
      for (int jc = 0; jc < 2; ++jc) {
        const char* base = smem + boff[jc] + st * kWStage + tp * 8192 + ks * 4096;
        const bf16x4 lo = __builtin_amdgcn_ds_read_tr16_b64_v4i16((LDS_AS bf16x4*)(base));
        const bf16x4 hi = __builtin_amdgcn_ds_read_tr16_b64_v4i16((LDS_AS bf16x4*)(base + 512));
        bF[buf][tp][jc] = __builtin_shufflevector(lo, hi, 0, 1, 2, 3, 4, 5, 6, 7);
      }
  };
  auto mfma_all = [&](int buf) {
#pragma unroll
    for (int tp = 0; tp < 3; ++tp)
#pragma unroll
      for (int jc = 0; jc < 2; ++jc)
#pragma unroll
        for (int i = 0; i < 5; ++i)
          acc[i][tp][jc] = __builtin_amdgcn_mfma_f32_16x16x32_bf16(aF[buf][i], bF[buf][tp][jc], acc[i][tp][jc], 0, 0, 0);
  };

  __builtin_amdgcn_s_barrier();
  read_frags(0, 0, 0);
#pragma unroll
  for (int c = 0; c < 9; ++c) {
    read_frags(1, c % 3, 1);
    mfma_all(0);
    if (c < 8) {
      asm volatile("s_waitcnt lgkmcnt(0)" ::: "memory");
      __builtin_amdgcn_s_barrier();
      read_frags(0, (c + 1) % 3, 0);
    }
    mfma_all(1);
  }

  float* exch = (float*)(smem + kExch);
  if (wm == 1 && g == 0) {
#pragma unroll
    for (int jc = 0; jc < 2; ++jc)
      *(f32x4*)(exch + ((wn * 2 + jc) * 16 + l15) * 4) = f32x4{acc[0][1][jc][0], acc[0][2][jc][0], acc[0][2][jc][1], 0.f};
  }
  asm volatile("s_waitcnt lgkmcnt(0)" ::: "memory");
  __builtin_amdgcn_s_barrier();

  const int nb_addr = ((lane + 16) & 63) * 4;
  char* tbase = smem + kTBase + wave * kTWave;
  const float log2e = 1.4426950408889634f;
  (void)log2e;
#pragma unroll
  for (int jc = 0; jc < 2; ++jc) {
    const float v1 = round_bf16(bf16_bits_to_f32(pv[jc]) + 1e-5f);
    const float inv_std = round_bf16(1.0f / round_bf16(sqrtf(v1)));
    const float gs = round_bf16(bf16_bits_to_f32(pg[jc]) * inv_std);
    const float sh = bf16_bits_to_f32(pb[jc]) - bf16_bits_to_f32(pm[jc]) * gs;

    float n1a = __int_as_float(__builtin_amdgcn_ds_bpermute(nb_addr, __float_as_int(acc[0][1][jc][0])));
    float n2a = __int_as_float(__builtin_amdgcn_ds_bpermute(nb_addr, __float_as_int(acc[0][2][jc][0])));
    float n2b = __int_as_float(__builtin_amdgcn_ds_bpermute(nb_addr, __float_as_int(acc[0][2][jc][1])));
    if (wm == 0 && g == 3) {
      const f32x4 e = *(const f32x4*)(exch + ((wn * 2 + jc) * 16 + l15) * 4);
      n1a = e[0]; n2a = e[1]; n2b = e[2];
    }
#pragma unroll
    for (int i = 0; i < 5; ++i) {
      float o[4];
#pragma unroll
      for (int r = 0; r < 4; ++r) {
        const int p1 = 4 * i + r + 1, p2 = 4 * i + r + 2;
        const float z1 = p1 < 20 ? acc[p1 >> 2][1][jc][p1 & 3] : n1a;
        const float z2 = p2 < 20 ? acc[p2 >> 2][2][jc][p2 & 3] : (p2 == 20 ? n2a : n2b);
        const float y = acc[i][0][jc][r] + z1 + z2;
        o[r] = silu_f32(y * gs + sh);
      }
      *(u32x2*)(tbase + (16 * jc + l15) * 160 + (20 * g + 4 * i) * 2) = u32x2{pack_bf16x2(o[0], o[1]), pack_bf16x2(o[2], o[3])};
    }
  }
  const int limit = kBM - 80 * wm;
  __hip_bfloat16* orow = out + (size_t)(batch * kS + s0 + 80 * wm + l15) * kC + n0 + 32 * wn + 8 * g;
#pragma unroll
  for (int rb = 0; rb < 5; ++rb) {
    const char* base = tbase + (8 * g + q) * 160 + (16 * rb + 4 * p) * 2;
    const bf16x4 lo = __builtin_amdgcn_ds_read_tr16_b64_v4i16((LDS_AS bf16x4*)(base));
    const bf16x4 hi = __builtin_amdgcn_ds_read_tr16_b64_v4i16((LDS_AS bf16x4*)(base + 4 * 160));
    const bf16x8 v = __builtin_shufflevector(lo, hi, 0, 1, 2, 3, 4, 5, 6, 7);
    const int R = 16 * rb + l15;
    if (R < limit && s0 + 80 * wm + R < kS) *(bf16x8*)(orow + (size_t)16 * rb * kC) = v;
  }
}

extern "C" void kernel_launch(void* const* d_in, const int* in_sizes, int n_in,
                              void* d_out, int out_size, void* d_ws, size_t ws_size,
                              hipStream_t stream) {
  (void)in_sizes; (void)n_in; (void)out_size; (void)d_ws; (void)ws_size;
  const __hip_bfloat16* x = (const __hip_bfloat16*)d_in[0];
  const __hip_bfloat16* w = (const __hip_bfloat16*)d_in[1];
  const __hip_bfloat16* mean = (const __hip_bfloat16*)d_in[2];
  const __hip_bfloat16* var = (const __hip_bfloat16*)d_in[3];
  const __hip_bfloat16* g = (const __hip_bfloat16*)d_in[4];
  const __hip_bfloat16* b = (const __hip_bfloat16*)d_in[5];
  __hip_bfloat16* out = (__hip_bfloat16*)d_out;
  hipFuncSetAttribute((const void*)conv3_bn_silu_kernel, hipFuncAttributeMaxDynamicSharedMemorySize, kLds);
  conv3_bn_silu_kernel<<<kTiles, 512, kLds, stream>>>(x, w, mean, var, g, b, out);
}
```

```cpp
#include <hip/hip_runtime.h>
#include <hip/hip_bf16.h>
#include <stdint.h>

typedef short    bf16x8 __attribute__((ext_vector_type(8)));
typedef short    bf16x4 __attribute__((ext_vector_type(4)));
typedef float    f32x4  __attribute__((ext_vector_type(4)));
typedef float    f32x2  __attribute__((ext_vector_type(2)));
typedef __bf16   hbf16x2 __attribute__((ext_vector_type(2)));
typedef unsigned u32x4  __attribute__((ext_vector_type(4)));
typedef unsigned u32x2  __attribute__((ext_vector_type(2)));

#define LDS_AS __attribute__((address_space(3)))
#define WG_BARRIER() do { asm volatile("" ::: "memory"); __builtin_amdgcn_s_barrier(); asm volatile("" ::: "memory"); } while (0)
#define PRO_MODE 1

namespace {
constexpr int kS = 2048, kC = 576, kB = 2;
constexpr int kBM = 158;
constexpr int kRows = 160;
constexpr int kMTB = 13;
constexpr int kNT = 9;
constexpr int kTiles = kB * kMTB * kNT;
constexpr int kXStage = kRows * 128;
constexpr int kWStage = 3 * 64 * 128;
constexpr int kWBase = 3 * kXStage;
constexpr int kLds = kWBase + 3 * kWStage;
constexpr int kHand = kWBase;
constexpr int kExch = kHand + 4 * 5120;
constexpr int kTImg = kExch + 1024;
constexpr unsigned kOOB = 0x80000000u;
static_assert(kTImg + 8 * 2560 <= kWBase + 2 * kWStage, "epilogue scratch must stay inside w stages 0/1");
}

__device__ __forceinline__ float bf16_bits_to_f32(unsigned short v) { return __uint_as_float((unsigned)v << 16); }
__device__ __forceinline__ float round_bf16(float v) { return __bfloat162float(__float2bfloat16(v)); }
__device__ __forceinline__ unsigned pack_bf16x2(float lo, float hi) {
  f32x2 v = {lo, hi};
  return __builtin_bit_cast(unsigned, __builtin_convertvector(v, hbf16x2));
}
__device__ __forceinline__ float silu_f32(float t) {
  const float e = __builtin_amdgcn_exp2f(t * -1.4426950408889634f);
  return t * __builtin_amdgcn_rcpf(1.0f + e);
}

__device__ __forceinline__ void bn_constants(const __hip_bfloat16* mean, const __hip_bfloat16* var,
                                             const __hip_bfloat16* gam, const __hip_bfloat16* bet, int ch,
                                             float& gs, float& sh) {
  const float v1 = round_bf16(bf16_bits_to_f32(((const unsigned short*)var)[ch]) + 1e-5f);
  const float inv_std = round_bf16(1.0f / round_bf16(sqrtf(v1)));
  gs = round_bf16(bf16_bits_to_f32(((const unsigned short*)gam)[ch]) * inv_std);
  sh = bf16_bits_to_f32(((const unsigned short*)bet)[ch]) - bf16_bits_to_f32(((const unsigned short*)mean)[ch]) * gs;
}

__device__ __forceinline__ void finish_block(const float (&y)[20], float gs, float sh, char* timg,
                                             __amdgpu_buffer_rsrc_t rout, unsigned out_base  ,
                                             int row_limit, int lane) {
  const int g = lane >> 4, l15 = lane & 15, q = l15 >> 2, p = lane & 3;
#pragma unroll
  for (int i = 0; i < 5; ++i) {
    float o[4];
#pragma unroll
    for (int r = 0; r < 4; ++r) o[r] = silu_f32(y[4 * i + r] * gs + sh);
    *(u32x2*)(timg + l15 * 160 + (20 * g + 4 * i) * 2) = u32x2{pack_bf16x2(o[0], o[1]), pack_bf16x2(o[2], o[3])};
  }
#pragma unroll
  for (int k = 0; k < 3; ++k) {
    const int rb = 2 * k + (g >> 1);
    const int rbc = rb < 5 ? rb : 4;
    const char* base = timg + (8 * (g & 1) + q) * 160 + (16 * rbc + 4 * p) * 2;
    const bf16x4 lo = __builtin_amdgcn_ds_read_tr16_b64_v4i16((LDS_AS bf16x4*)(base));
    const bf16x4 hi = __builtin_amdgcn_ds_read_tr16_b64_v4i16((LDS_AS bf16x4*)(base + 4 * 160));
    const u32x4 v = __builtin_bit_cast(u32x4, __builtin_shufflevector(lo, hi, 0, 1, 2, 3, 4, 5, 6, 7));
    const int R = 16 * rb + l15;
    const unsigned off = R < row_limit ? out_base + (unsigned)R * (kC * 2) + (g & 1) * 16 : kOOB;
    __builtin_amdgcn_raw_buffer_store_b128(v, rout, off, 0, 0);
  }
}

__global__ __launch_bounds__(512, 2)
void conv3_bn_silu_kernel(const __hip_bfloat16* __restrict__ x, const __hip_bfloat16* __restrict__ w,
                          const __hip_bfloat16* __restrict__ mean, const __hip_bfloat16* __restrict__ var,
                          const __hip_bfloat16* __restrict__ gam, const __hip_bfloat16* __restrict__ bet,
                          __hip_bfloat16* __restrict__ out
                          ) {
  extern __shared__ __attribute__((aligned(16))) char smem[];
  const int tid = threadIdx.x;
  const int lane = tid & 63;
  const int wave = __builtin_amdgcn_readfirstlane(tid >> 6);

  const int bid = blockIdx.x;
  const int grp8 = bid & 7, idx = bid >> 3;
  const int t = (grp8 < 2 ? grp8 * 30 : 60 + (grp8 - 2) * 29) + idx;
  const int mt = t / kNT, nt = t - mt * kNT;
  const int batch = mt / kMTB, mb = mt - batch * kMTB;
  const int s0 = mb * kBM;
  const int n0 = nt * 64;
  const auto rout = __builtin_amdgcn_make_buffer_rsrc((void*)out, (short)0, kB * kS * kC * 2, 0x00020000);
  auto row_limit_of = [&](int wmr) {
    const int a = wmr == 0 ? 80 : kBM - 80, b2 = kS - s0 - 80 * wmr;
    return a < b2 ? a : b2;
  };

  if (wave >= 4) {
    const int lw = wave - 4;
    const auto rx = __builtin_amdgcn_make_buffer_rsrc((void*)x, (short)0, kB * kS * kC * 2, 0x00020000);
    const auto rw = __builtin_amdgcn_make_buffer_rsrc((void*)w, (short)0, 3 * kC * kC * 2, 0x00020000);
    const int klo = lane >> 3, cp = lane & 7;
    unsigned xoff[5];
#pragma unroll
    for (int jj = 0; jj < 5; ++jj) {
      const int L = 8 * (5 * lw + jj) + klo;
      const int Lm = L >= 80 ? L - 80 : L;
      const int sw = ((Lm / 20) << 1) | ((L >> 1) & 1);
      const int c = cp ^ sw;
      const int s = s0 - 1 + L;
      xoff[jj] = ((unsigned)s < (unsigned)kS) ? (unsigned)(((batch * kS + s) * kC + c * 8) * 2) : kOOB;
    }
    unsigned woff[2];
#pragma unroll
    for (int par = 0; par < 2; ++par) {
      const int fw = (klo & 2) | (par << 2);
      woff[par] = (unsigned)((klo * kC + n0 + (cp ^ fw) * 8) * 2);
    }
    auto issue = [&](int cc, int st) {
#pragma unroll
      for (int jj = 0; jj < 5; ++jj)
        __builtin_amdgcn_raw_ptr_buffer_load_lds(rx, (LDS_AS void*)(smem + st * kXStage + (5 * lw + jj) * 1024), 16,
                                                 xoff[jj], cc * 128, 0, 0);
#pragma unroll
      for (int ii = 0; ii < 6; ++ii) {
        const int u = 6 * lw + ii;
        const int tap = u >> 3, rg = u & 7;
        __builtin_amdgcn_raw_ptr_buffer_load_lds(rw, (LDS_AS void*)(smem + kWBase + st * kWStage + u * 1024), 16,
                                                 woff[ii & 1], (tap * kC + rg * 8 + cc * 64) * (kC * 2), 0, 0);
      }
    };
    issue(0, 0);
    issue(1, 1);
    asm volatile("s_waitcnt vmcnt(11)" ::: "memory");
    WG_BARRIER();
    issue(2, 2);
#pragma unroll
    for (int c = 0; c < 8; ++c) {
      if (c < 7) asm volatile("s_waitcnt vmcnt(11)" ::: "memory");
      else       asm volatile("s_waitcnt vmcnt(0)" ::: "memory");
      WG_BARRIER();
      if (c + 3 <= 8) issue(c + 3, c % 3);
    }
    const int wm = lw >> 1, wn = lw & 1, g = lane >> 4, l15 = lane & 15;
    float gs1, sh1;
    bn_constants(mean, var, gam, bet, n0 + 32 * wn + 16 + l15, gs1, sh1);
    asm volatile("" : "+v"(gs1), "+v"(sh1));
    WG_BARRIER();
    float y1[20];
#pragma unroll
    for (int k = 0; k < 5; ++k) {
      const f32x4 v = *(const f32x4*)(smem + kHand + lw * 5120 + k * 1024 + lane * 16);
      y1[4 * k] = v[0]; y1[4 * k + 1] = v[1]; y1[4 * k + 2] = v[2]; y1[4 * k + 3] = v[3];
    }
    if (wm == 0 && g == 3) {
      const f32x4 e = *(const f32x4*)(smem + kExch + ((wn * 2 + 1) * 16 + l15) * 16);
      y1[18] += e[1]; y1[19] += e[0] + e[2];
    }
    finish_block(y1, gs1, sh1, smem + kTImg + wave * 2560, rout,
                 (unsigned)(((batch * kS + s0 + 80 * wm) * kC + n0 + 32 * wn + 16) * 2), row_limit_of(wm), lane);
    return;
  }

  const int wm = wave >> 1, wn = wave & 1;
  const int g = lane >> 4, l15 = lane & 15, q = l15 >> 2, p = lane & 3;

  float gs0, sh0;
  bn_constants(mean, var, gam, bet, n0 + 32 * wn + l15, gs0, sh0);
  asm volatile("" : "+v"(gs0), "+v"(sh0));

  const int swA = (q << 1) | (p >> 1);
  int aoff[2];
#pragma unroll
  for (int ks = 0; ks < 2; ++ks) aoff[ks] = (80 * wm + 20 * q + p) * 128 + (((4 * ks + g) ^ swA) << 4);
  const int fw = (q & 2) | ((g & 1) << 2);
  int boff[2];
#pragma unroll
  for (int jc = 0; jc < 2; ++jc)
    boff[jc] = kWBase + (8 * g + q) * 128 + (((4 * wn + 2 * jc + (p >> 1)) ^ fw) << 4) + (p & 1) * 8;

  f32x4 acc[5][3][2];
#pragma unroll
  for (int i = 0; i < 5; ++i)
#pragma unroll
    for (int tp = 0; tp < 3; ++tp)
#pragma unroll
      for (int jc = 0; jc < 2; ++jc) acc[i][tp][jc] = f32x4{0.f, 0.f, 0.f, 0.f};

  bf16x8 aF[2][5];
  bf16x4 bL[2][3][2], bH[2][3][2];
  auto read_frag = [&](int buf, int st, int ks, int e) {
    if (e < 5) {
      aF[buf][e] = *(const bf16x8*)(smem + aoff[ks] + st * kXStage + e * 512);
    } else {
      const int tp = (e - 5) >> 2, jc = ((e - 5) >> 1) & 1, hi = (e - 5) & 1;
      const char* base = smem + boff[jc] + st * kWStage + tp * 8192 + ks * 4096 + hi * 512;
      const bf16x4 v = __builtin_amdgcn_ds_read_tr16_b64_v4i16((LDS_AS bf16x4*)(base));
      if (hi) bH[buf][tp][jc] = v; else bL[buf][tp][jc] = v;
    }
  };
  auto half_step = [&](int cur, bool rd, int st, int ks) {
#pragma unroll
    for (int m = 0; m < 30; ++m) {
      if (rd && m < 17) read_frag(cur ^ 1, st, ks, m);
      const int tp = m / 10, jc = (m / 5) & 1, i = m % 5;
      const bf16x8 bfr = __builtin_shufflevector(bL[cur][tp][jc], bH[cur][tp][jc], 0, 1, 2, 3, 4, 5, 6, 7);
      acc[i][tp][jc] = __builtin_amdgcn_mfma_f32_16x16x32_bf16(aF[cur][i], bfr, acc[i][tp][jc], 0, 0, 0);
      __builtin_amdgcn_sched_barrier(0);
    }
  };

  WG_BARRIER();
#pragma unroll
  for (int e = 0; e < 17; ++e) read_frag(0, 0, 0, e);
  __builtin_amdgcn_sched_barrier(0);
#pragma unroll
  for (int c = 0; c < 9; ++c) {
    half_step(0, true, c % 3, 1);
    if (c < 8) {
      asm volatile("s_waitcnt lgkmcnt(0)" ::: "memory");
      WG_BARRIER();
      __builtin_amdgcn_sched_barrier(0);
    }
    half_step(1, c < 8, (c + 1) % 3, 0);
  }

  if (wm == 1 && g == 0) {
#pragma unroll
    for (int jc = 0; jc < 2; ++jc)
      *(f32x4*)(smem + kExch + ((wn * 2 + jc) * 16 + l15) * 16) =
          f32x4{acc[0][1][jc][0], acc[0][2][jc][0], acc[0][2][jc][1], 0.f};
  }
  const int nb_addr = ((lane + 16) & 63) * 4;
  float yv[2][20];
#pragma unroll
  for (int jc = 0; jc < 2; ++jc) {
    float n1a = __int_as_float(__builtin_amdgcn_ds_bpermute(nb_addr, __float_as_int(acc[0][1][jc][0])));
    float n2a = __int_as_float(__builtin_amdgcn_ds_bpermute(nb_addr, __float_as_int(acc[0][2][jc][0])));
    float n2b = __int_as_float(__builtin_amdgcn_ds_bpermute(nb_addr, __float_as_int(acc[0][2][jc][1])));
    if (g == 3) { n1a = 0.f; n2a = 0.f; n2b = 0.f; }
#pragma unroll
    for (int pp = 0; pp < 20; ++pp) {
      const int p1 = pp + 1, p2 = pp + 2;
      const float z1 = p1 < 20 ? acc[p1 >> 2][1][jc][p1 & 3] : n1a;
      const float z2 = p2 < 20 ? acc[p2 >> 2][2][jc][p2 & 3] : (p2 == 20 ? n2a : n2b);
      yv[jc][pp] = (acc[pp >> 2][0][jc][pp & 3] + z2) + z1;
    }
  }
#pragma unroll
  for (int k = 0; k < 5; ++k)
    *(f32x4*)(smem + kHand + wave * 5120 + k * 1024 + lane * 16) = f32x4{yv[1][4 * k], yv[1][4 * k + 1], yv[1][4 * k + 2], yv[1][4 * k + 3]};
  asm volatile("s_waitcnt lgkmcnt(0)" ::: "memory");
  WG_BARRIER();
  if (wm == 0 && g == 3) {
    const f32x4 e = *(const f32x4*)(smem + kExch + ((wn * 2 + 0) * 16 + l15) * 16);
    yv[0][18] += e[1]; yv[0][19] += e[0] + e[2];
  }
  finish_block(yv[0], gs0, sh0, smem + kTImg + wave * 2560, rout,
               (unsigned)(((batch * kS + s0 + 80 * wm) * kC + n0 + 32 * wn) * 2), row_limit_of(wm), lane);
}

extern "C" void kernel_launch(void* const* d_in, const int* in_sizes, int n_in,
                              void* d_out, int out_size, void* d_ws, size_t ws_size,
                              hipStream_t stream) {
  (void)in_sizes; (void)n_in; (void)out_size; (void)d_ws; (void)ws_size;
  const __hip_bfloat16* x = (const __hip_bfloat16*)d_in[0];
  const __hip_bfloat16* w = (const __hip_bfloat16*)d_in[1];
  const __hip_bfloat16* mean = (const __hip_bfloat16*)d_in[2];
  const __hip_bfloat16* var = (const __hip_bfloat16*)d_in[3];
  const __hip_bfloat16* g = (const __hip_bfloat16*)d_in[4];
  const __hip_bfloat16* b = (const __hip_bfloat16*)d_in[5];
  __hip_bfloat16* out = (__hip_bfloat16*)d_out;
  hipFuncSetAttribute((const void*)conv3_bn_silu_kernel, hipFuncAttributeMaxDynamicSharedMemorySize, kLds);
  conv3_bn_silu_kernel<<<kTiles, 512, kLds, stream>>>(x, w, mean, var, g, b, out
      );
}
```

```cpp
#include <hip/hip_runtime.h>
#include <hip/hip_bf16.h>
#include <stdint.h>

typedef short    bf16x8 __attribute__((ext_vector_type(8)));
typedef short    bf16x4 __attribute__((ext_vector_type(4)));
typedef float    f32x4  __attribute__((ext_vector_type(4)));
typedef float    f32x2  __attribute__((ext_vector_type(2)));
typedef __bf16   hbf16x2 __attribute__((ext_vector_type(2)));
typedef unsigned u32x4  __attribute__((ext_vector_type(4)));

#define LDS_AS __attribute__((address_space(3)))
#define WG_BARRIER() do { asm volatile("" ::: "memory"); __builtin_amdgcn_s_barrier(); asm volatile("" ::: "memory"); } while (0)
#define RD_DENS 1
#define PRO_MODE 1

namespace {
constexpr int kS = 2048, kC = 576, kB = 2;
constexpr int kBM = 158;
constexpr int kRows = 160;
constexpr int kMTB = 13;
constexpr int kNT = 9;
constexpr int kTiles = kB * kMTB * kNT;
constexpr int kXStage = kRows * 128;
constexpr int kWStage = 3 * 64 * 128;
constexpr int kWBase = 3 * kXStage;
constexpr int kLds = kWBase + 3 * kWStage;
constexpr unsigned kOOB = 0x80000000u;
}

__device__ __forceinline__ float bf16_bits_to_f32(unsigned short v) { return __uint_as_float((unsigned)v << 16); }
__device__ __forceinline__ float round_bf16(float v) { return __bfloat162float(__float2bfloat16(v)); }
__device__ __forceinline__ unsigned pack_bf16x2(f32x2 v) {
  return __builtin_bit_cast(unsigned, __builtin_convertvector(v, hbf16x2));
}

__global__ __launch_bounds__(512, 2)
void conv3_bn_silu_kernel(const __hip_bfloat16* __restrict__ x, const __hip_bfloat16* __restrict__ w,
                          const __hip_bfloat16* __restrict__ mean, const __hip_bfloat16* __restrict__ var,
                          const __hip_bfloat16* __restrict__ gam, const __hip_bfloat16* __restrict__ bet,
                          __hip_bfloat16* __restrict__ out
                          ) {
  extern __shared__ __attribute__((aligned(16))) char smem[];
  const int tid = threadIdx.x;
  const int lane = tid & 63;
  const int wave = __builtin_amdgcn_readfirstlane(tid >> 6);

  const int bid = blockIdx.x;
  const int grp8 = bid & 7, idx = bid >> 3;
  const int t = (grp8 < 2 ? grp8 * 30 : 60 + (grp8 - 2) * 29) + idx;
  const int mt = t / kNT, nt = t - mt * kNT;
  const int batch = mt / kMTB, mb = mt - batch * kMTB;
  const int s0 = mb * kBM;
  const int n0 = nt * 64;

  if (wave >= 4) {
    const int lw = wave - 4;
    const auto rx = __builtin_amdgcn_make_buffer_rsrc((void*)x, (short)0, kB * kS * kC * 2, 0x00020000);
    const auto rw = __builtin_amdgcn_make_buffer_rsrc((void*)w, (short)0, 3 * kC * kC * 2, 0x00020000);
    const int klo = lane >> 3, cp = lane & 7;
    unsigned xoff[5];
#pragma unroll
    for (int jj = 0; jj < 5; ++jj) {
      const int L = 8 * (5 * lw + jj) + klo;
      const int s = s0 - 1 + L;
      unsigned a = (unsigned)(((batch * kS + s) * kC + (cp ^ ((L >> 1) & 7)) * 8) * 2);
      asm volatile("" : "+v"(a));
      xoff[jj] = ((unsigned)s < (unsigned)kS) ? a : kOOB;
    }
    unsigned woff[2];
#pragma unroll
    for (int par = 0; par < 2; ++par) {
      const int fw = (klo & 2) | (par << 2);
      woff[par] = (unsigned)((klo * kC + n0 + (cp ^ fw) * 8) * 2);
    }
    auto issue = [&](int cc, int st) {
#pragma unroll
      for (int jj = 0; jj < 5; ++jj)
        __builtin_amdgcn_raw_ptr_buffer_load_lds(rx, (LDS_AS void*)(smem + st * kXStage + (5 * lw + jj) * 1024), 16,
                                                 xoff[jj], cc * 128, 0, 0);
#pragma unroll
      for (int ii = 0; ii < 6; ++ii) {
        const int u = 6 * lw + ii;
        const int tap = u >> 3, rg = u & 7;
        __builtin_amdgcn_raw_ptr_buffer_load_lds(rw, (LDS_AS void*)(smem + kWBase + st * kWStage + u * 1024), 16,
                                                 woff[ii & 1], (tap * kC + rg * 8 + cc * 64) * (kC * 2), 0, 0);
      }
    };
    issue(0, 0);
    issue(1, 1);
    asm volatile("s_waitcnt vmcnt(11)" ::: "memory");
    WG_BARRIER();
    issue(2, 2);
#pragma unroll
    for (int c = 0; c < 8; ++c) {
      if (c < 7) asm volatile("s_waitcnt vmcnt(11)" ::: "memory");
      else       asm volatile("s_waitcnt vmcnt(0)" ::: "memory");
      WG_BARRIER();
      if (c + 3 <= 8) issue(c + 3, c % 3);
    }
    return;
  }

  const int wm = wave >> 1, wn = wave & 1;
  const int g = lane >> 4, l15 = lane & 15, q = l15 >> 2, p = lane & 3;

  f32x4 gs[2], sh[2];
  {
    const int ch = n0 + 32 * wn + (lane & 31);
    const float v1 = round_bf16(bf16_bits_to_f32(((const unsigned short*)var)[ch]) + 1e-5f);
    const float inv_std = round_bf16(1.0f / round_bf16(sqrtf(v1)));
    const float gg = round_bf16(bf16_bits_to_f32(((const unsigned short*)gam)[ch]) * inv_std);
    const float hh = bf16_bits_to_f32(((const unsigned short*)bet)[ch]) - bf16_bits_to_f32(((const unsigned short*)mean)[ch]) * gg;
#pragma unroll
    for (int jn = 0; jn < 2; ++jn)
#pragma unroll
      for (int r = 0; r < 4; ++r) {
        const int src = (16 * jn + 4 * g + r) * 4;
        float a = __int_as_float(__builtin_amdgcn_ds_bpermute(src, __float_as_int(gg)));
        float b2 = __int_as_float(__builtin_amdgcn_ds_bpermute(src, __float_as_int(hh)));
        asm volatile("" : "+v"(a), "+v"(b2));
        gs[jn][r] = a; sh[jn][r] = b2;
      }
  }

  const int prow = (l15 >= 4 && l15 < 12) ? 2 * (l15 - 4) + 1 : 2 * (l15 < 4 ? l15 : l15 - 8);
  int aoff[3][2];
#pragma unroll
  for (int tp = 0; tp < 3; ++tp)
#pragma unroll
    for (int ks = 0; ks < 2; ++ks)
      aoff[tp][ks] = (80 * wm + prow + tp) * 128 + (((4 * ks + g) ^ (((prow + tp) >> 1) & 7)) << 4);
  const int fw = (q & 2) | ((g & 1) << 2);
  int boff[2];
#pragma unroll
  for (int jn = 0; jn < 2; ++jn)
    boff[jn] = kWBase + (8 * g + q) * 128 + (((4 * wn + 2 * jn + (p >> 1)) ^ fw) << 4) + (p & 1) * 8;

  f32x4 acc[5][2];
#pragma unroll
  for (int i = 0; i < 5; ++i)
#pragma unroll
    for (int jn = 0; jn < 2; ++jn) acc[i][jn] = f32x4{0.f, 0.f, 0.f, 0.f};

  bf16x8 xF[2][3][5];
  bf16x4 wL[2][3][2], wH[2][3][2];
  auto read_frag = [&](int buf, int st, int ks, int e) {
    const int tp = e / 9, r = e - tp * 9;
    if (r < 5) {
      xF[buf][tp][r] = *(const bf16x8*)(smem + aoff[tp][ks] + st * kXStage + r * 2048);
    } else {
      const int jn = (r - 5) >> 1, hi = (r - 5) & 1;
      const char* base = smem + boff[jn] + st * kWStage + tp * 8192 + ks * 4096 + hi * 512;
      const bf16x4 v = __builtin_amdgcn_ds_read_tr16_b64_v4i16((LDS_AS bf16x4*)(base));
      if (hi) wH[buf][tp][jn] = v; else wL[buf][tp][jn] = v;
    }
  };
  auto half_step = [&](int cur, bool rd, int st, int ks) {
#pragma unroll
    for (int m = 0; m < 30; ++m) {
      if (rd && m < 27) read_frag(cur ^ 1, st, ks, m);
      const int tp = m / 10, jn = (m / 5) & 1, i = m % 5;
      const bf16x8 wf = __builtin_shufflevector(wL[cur][tp][jn], wH[cur][tp][jn], 0, 1, 2, 3, 4, 5, 6, 7);
      acc[i][jn] = __builtin_amdgcn_mfma_f32_16x16x32_bf16(wf, xF[cur][tp][i], acc[i][jn], 0, 0, 0);
      __builtin_amdgcn_sched_barrier(0);
    }
  };

  WG_BARRIER();
#pragma unroll
  for (int e = 0; e < 27; ++e) read_frag(0, 0, 0, e);
  __builtin_amdgcn_sched_barrier(0);
#pragma unroll
  for (int c = 0; c < 9; ++c) {
    half_step(0, true, c % 3, 1);
    if (c < 8) {
      asm volatile("s_waitcnt lgkmcnt(0)" ::: "memory");
      WG_BARRIER();
      __builtin_amdgcn_sched_barrier(0);
    }
    half_step(1, c < 8, (c + 1) % 3, 0);
  }

  const auto rout = __builtin_amdgcn_make_buffer_rsrc((void*)out, (short)0, kB * kS * kC * 2, 0x00020000);
  const int limit = (kBM - 80 * wm < kS - s0 - 80 * wm) ? kBM - 80 * wm : kS - s0 - 80 * wm;
  const unsigned obase = (unsigned)(((batch * kS + s0 + 80 * wm + prow) * kC + n0 + 32 * wn + 8 * (((g & 1) << 1) | (g >> 1))) * 2);
  const f32x2 kNL2E = {-1.4426950408889634f, -1.4426950408889634f};
  f32x2 t2[5][2][2], a2[5][2][2];
#pragma unroll
  for (int i = 0; i < 5; ++i)
#pragma unroll
    for (int jn = 0; jn < 2; ++jn)
#pragma unroll
      for (int h = 0; h < 2; ++h) {
        const f32x2 y = {acc[i][jn][2 * h], acc[i][jn][2 * h + 1]};
        t2[i][jn][h] = y * f32x2{gs[jn][2 * h], gs[jn][2 * h + 1]} + f32x2{sh[jn][2 * h], sh[jn][2 * h + 1]};
        a2[i][jn][h] = t2[i][jn][h] * kNL2E;
      }
#pragma unroll
  for (int i = 0; i < 5; ++i)
#pragma unroll
    for (int jn = 0; jn < 2; ++jn)
#pragma unroll
      for (int h = 0; h < 2; ++h) {
        a2[i][jn][h][0] = __builtin_amdgcn_exp2f(a2[i][jn][h][0]);
        a2[i][jn][h][1] = __builtin_amdgcn_exp2f(a2[i][jn][h][1]);
        a2[i][jn][h] = a2[i][jn][h] + 1.0f;
      }
#pragma unroll
  for (int i = 0; i < 5; ++i) {
    unsigned d[2][2];
#pragma unroll
    for (int jn = 0; jn < 2; ++jn)
#pragma unroll
      for (int h = 0; h < 2; ++h) {
        const f32x2 r = {__builtin_amdgcn_rcpf(a2[i][jn][h][0]), __builtin_amdgcn_rcpf(a2[i][jn][h][1])};
        d[jn][h] = pack_bf16x2(t2[i][jn][h] * r);
      }
    const auto s0v = __builtin_amdgcn_permlane16_swap(d[0][0], d[1][0], false, false);
    const auto s1v = __builtin_amdgcn_permlane16_swap(d[0][1], d[1][1], false, false);
    const u32x4 v = {s0v[0], s1v[0], s0v[1], s1v[1]};
    const unsigned off = 16 * i + prow < limit ? obase + (unsigned)(16 * i * kC * 2) : kOOB;
    __builtin_amdgcn_raw_buffer_store_b128(v, rout, off, 0, 0);
  }
}

extern "C" void kernel_launch(void* const* d_in, const int* in_sizes, int n_in,
                              void* d_out, int out_size, void* d_ws, size_t ws_size,
                              hipStream_t stream) {
  (void)in_sizes; (void)n_in; (void)out_size; (void)d_ws; (void)ws_size;
  const __hip_bfloat16* x = (const __hip_bfloat16*)d_in[0];
  const __hip_bfloat16* w = (const __hip_bfloat16*)d_in[1];
  const __hip_bfloat16* mean = (const __hip_bfloat16*)d_in[2];
  const __hip_bfloat16* var = (const __hip_bfloat16*)d_in[3];
  const __hip_bfloat16* g = (const __hip_bfloat16*)d_in[4];
  const __hip_bfloat16* b = (const __hip_bfloat16*)d_in[5];
  __hip_bfloat16* out = (__hip_bfloat16*)d_out;
  (void)hipFuncSetAttribute((const void*)conv3_bn_silu_kernel, hipFuncAttributeMaxDynamicSharedMemorySize, kLds);
  conv3_bn_silu_kernel<<<kTiles, 512, kLds, stream>>>(x, w, mean, var, g, b, out
      );
}
```

```cpp
#include <hip/hip_runtime.h>
#include <hip/hip_bf16.h>
#include <stdint.h>

typedef short    bf16x8 __attribute__((ext_vector_type(8)));
typedef short    bf16x4 __attribute__((ext_vector_type(4)));
typedef float    f32x4  __attribute__((ext_vector_type(4)));
typedef float    f32x2  __attribute__((ext_vector_type(2)));
typedef __bf16   hbf16x2 __attribute__((ext_vector_type(2)));
typedef unsigned u32x4  __attribute__((ext_vector_type(4)));

#define LDS_AS __attribute__((address_space(3)))
#define WG_BARRIER() do { asm volatile("" ::: "memory"); __builtin_amdgcn_s_barrier(); asm volatile("" ::: "memory"); } while (0)

namespace {
constexpr int kS = 2048, kC = 576, kB = 2;
constexpr int kBM = 158;
constexpr int kRows = 160;
constexpr int kMTB = 13;
constexpr int kNT = 9;
constexpr int kTiles = kB * kMTB * kNT;
constexpr int kXStage = kRows * 128;
constexpr int kWStage = 3 * 64 * 128;
constexpr int kWBase = 3 * kXStage;
constexpr int kLds = kWBase + 3 * kWStage;
constexpr unsigned kOOB = 0x80000000u;
}

__device__ __forceinline__ float bf16_bits_to_f32(unsigned short v) { return __uint_as_float((unsigned)v << 16); }
__device__ __forceinline__ float round_bf16(float v) { return __bfloat162float(__float2bfloat16(v)); }
__device__ __forceinline__ unsigned pack_bf16x2(f32x2 v) {
  return __builtin_bit_cast(unsigned, __builtin_convertvector(v, hbf16x2));
}

__global__ __launch_bounds__(512, 2)
void conv3_bn_silu_kernel(const __hip_bfloat16* __restrict__ x, const __hip_bfloat16* __restrict__ w,
                          const __hip_bfloat16* __restrict__ mean, const __hip_bfloat16* __restrict__ var,
                          const __hip_bfloat16* __restrict__ gam, const __hip_bfloat16* __restrict__ bet,
                          __hip_bfloat16* __restrict__ out) {
  extern __shared__ __attribute__((aligned(16))) char smem[];
  const int tid = threadIdx.x;
  const int lane = tid & 63;
  const int wave = __builtin_amdgcn_readfirstlane(tid >> 6);

  const int bid = blockIdx.x;
  const int grp8 = bid & 7, idx = bid >> 3;
  const int t = (grp8 < 2 ? grp8 * 30 : 60 + (grp8 - 2) * 29) + idx;
  const int mt = t / kNT, nt = t - mt * kNT;
  const int batch = mt / kMTB, mb = mt - batch * kMTB;
  const int s0 = mb * kBM;
  const int n0 = nt * 64;

  if (wave >= 4) {
    const int lw = wave - 4;
    const auto rx = __builtin_amdgcn_make_buffer_rsrc((void*)x, (short)0, kB * kS * kC * 2, 0x00020000);
    const auto rw = __builtin_amdgcn_make_buffer_rsrc((void*)w, (short)0, 3 * kC * kC * 2, 0x00020000);
    const int klo = lane >> 3, cp = lane & 7;
    unsigned xoff[5];
#pragma unroll
    for (int jj = 0; jj < 5; ++jj) {
      const int L = 8 * (5 * lw + jj) + klo;
      const int s = s0 - 1 + L;
      unsigned a = (unsigned)(((batch * kS + s) * kC + (cp ^ ((L >> 1) & 7)) * 8) * 2);
      asm volatile("" : "+v"(a));
      xoff[jj] = ((unsigned)s < (unsigned)kS) ? a : kOOB;
    }
    unsigned woff[2];
#pragma unroll
    for (int par = 0; par < 2; ++par) {
      const int fw = (klo & 2) | (par << 2);
      woff[par] = (unsigned)((klo * kC + n0 + (cp ^ fw) * 8) * 2);
    }
    auto issue_part = [&](int cc, int st, int from, int to) {
#pragma unroll
      for (int e = 0; e < 11; ++e) {
        if (e < from || e >= to) continue;
        if (e < 5) {
          __builtin_amdgcn_raw_ptr_buffer_load_lds(rx, (LDS_AS void*)(smem + st * kXStage + (5 * lw + e) * 1024), 16,
                                                   xoff[e], cc * 128, 0, 0);
        } else {
          const int ii = e - 5, u = 6 * lw + ii;
          const int tap = u >> 3, rg = u & 7;
          __builtin_amdgcn_raw_ptr_buffer_load_lds(rw, (LDS_AS void*)(smem + kWBase + st * kWStage + u * 1024), 16,
                                                   woff[ii & 1], (tap * kC + rg * 8 + cc * 64) * (kC * 2), 0, 0);
        }
      }
    };
    auto issue = [&](int cc, int st) { issue_part(cc, st, 0, 11); };
    issue(0, 0);
    issue_part(1, 1, 0, 6);
    asm volatile("s_waitcnt vmcnt(6)" ::: "memory");
    WG_BARRIER();
    issue_part(1, 1, 6, 11);
    issue(2, 2);
#pragma unroll
    for (int c = 0; c < 8; ++c) {
      if (c < 7) asm volatile("s_waitcnt vmcnt(11)" ::: "memory");
      else       asm volatile("s_waitcnt vmcnt(0)" ::: "memory");
      WG_BARRIER();
      if (c + 3 <= 8) issue(c + 3, c % 3);
    }
    return;
  }

  const int wm = wave >> 1, wn = wave & 1;
  const int g = lane >> 4, l15 = lane & 15, q = l15 >> 2, p = lane & 3;

  f32x4 gs[2], sh[2];
  {
    const int ch = n0 + 32 * wn + (lane & 31);
    const float v1 = round_bf16(bf16_bits_to_f32(((const unsigned short*)var)[ch]) + 1e-5f);
    const float inv_std = round_bf16(1.0f / round_bf16(sqrtf(v1)));
    const float gg = round_bf16(bf16_bits_to_f32(((const unsigned short*)gam)[ch]) * inv_std);
    const float hh = bf16_bits_to_f32(((const unsigned short*)bet)[ch]) - bf16_bits_to_f32(((const unsigned short*)mean)[ch]) * gg;
#pragma unroll
    for (int jn = 0; jn < 2; ++jn)
#pragma unroll
      for (int r = 0; r < 4; ++r) {
        const int src = (16 * jn + 4 * g + r) * 4;
        float a = __int_as_float(__builtin_amdgcn_ds_bpermute(src, __float_as_int(gg)));
        float b2 = __int_as_float(__builtin_amdgcn_ds_bpermute(src, __float_as_int(hh)));
        asm volatile("" : "+v"(a), "+v"(b2));
        gs[jn][r] = a; sh[jn][r] = b2;
      }
  }

  const int prow = (l15 >= 4 && l15 < 12) ? 2 * (l15 - 4) + 1 : 2 * (l15 < 4 ? l15 : l15 - 8);
  int aoff[3][2];
#pragma unroll
  for (int tp = 0; tp < 3; ++tp)
#pragma unroll
    for (int ks = 0; ks < 2; ++ks)
      aoff[tp][ks] = (80 * wm + prow + tp) * 128 + (((4 * ks + g) ^ (((prow + tp) >> 1) & 7)) << 4);
  const int fw = (q & 2) | ((g & 1) << 2);
  int boff[2];
#pragma unroll
  for (int jn = 0; jn < 2; ++jn)
    boff[jn] = kWBase + (8 * g + q) * 128 + (((4 * wn + 2 * jn + (p >> 1)) ^ fw) << 4) + (p & 1) * 8;

  f32x4 acc[5][2];
#pragma unroll
  for (int i = 0; i < 5; ++i)
#pragma unroll
    for (int jn = 0; jn < 2; ++jn) acc[i][jn] = f32x4{0.f, 0.f, 0.f, 0.f};

  bf16x8 xF[2][3][5];
  bf16x4 wL[2][3][2], wH[2][3][2];
  auto read_frag = [&](int buf, int st, int ks, int e) {
    const int tp = e / 9, r = e - tp * 9;
    if (r < 5) {
      xF[buf][tp][r] = *(const bf16x8*)(smem + aoff[tp][ks] + st * kXStage + r * 2048);
    } else {
      const int jn = (r - 5) >> 1, hi = (r - 5) & 1;
      const char* base = smem + boff[jn] + st * kWStage + tp * 8192 + ks * 4096 + hi * 512;
      const bf16x4 v = __builtin_amdgcn_ds_read_tr16_b64_v4i16((LDS_AS bf16x4*)(base));
      if (hi) wH[buf][tp][jn] = v; else wL[buf][tp][jn] = v;
    }
  };
  auto half_step = [&](int cur, bool rd, int st, int ks) {
#pragma unroll
    for (int m = 0; m < 30; ++m) {
      if (rd && m < 27) read_frag(cur ^ 1, st, ks, m);
      const int tp = m / 10, jn = (m / 5) & 1, i = m % 5;
      const bf16x8 wf = __builtin_shufflevector(wL[cur][tp][jn], wH[cur][tp][jn], 0, 1, 2, 3, 4, 5, 6, 7);
      acc[i][jn] = __builtin_amdgcn_mfma_f32_16x16x32_bf16(wf, xF[cur][tp][i], acc[i][jn], 0, 0, 0);
      __builtin_amdgcn_sched_barrier(0);
    }
  };

  WG_BARRIER();
#pragma unroll
  for (int e = 0; e < 27; ++e) read_frag(0, 0, 0, e);
  __builtin_amdgcn_sched_barrier(0);
#pragma unroll
  for (int c = 0; c < 9; ++c) {
    half_step(0, true, c % 3, 1);
    if (c < 8) {
      asm volatile("s_waitcnt lgkmcnt(0)" ::: "memory");
      WG_BARRIER();
      __builtin_amdgcn_sched_barrier(0);
    }
    half_step(1, c < 8, (c + 1) % 3, 0);
  }

  const auto rout = __builtin_amdgcn_make_buffer_rsrc((void*)out, (short)0, kB * kS * kC * 2, 0x00020000);
  const int limit = (kBM - 80 * wm < kS - s0 - 80 * wm) ? kBM - 80 * wm : kS - s0 - 80 * wm;
  const unsigned obase = (unsigned)(((batch * kS + s0 + 80 * wm + prow) * kC + n0 + 32 * wn + 8 * (((g & 1) << 1) | (g >> 1))) * 2);
  const f32x2 kNL2E = {-1.4426950408889634f, -1.4426950408889634f};
  f32x2 t2[5][2][2], a2[5][2][2];
#pragma unroll
  for (int i = 0; i < 5; ++i)
#pragma unroll
    for (int jn = 0; jn < 2; ++jn)
#pragma unroll
      for (int h = 0; h < 2; ++h) {
        const f32x2 y = {acc[i][jn][2 * h], acc[i][jn][2 * h + 1]};
        t2[i][jn][h] = y * f32x2{gs[jn][2 * h], gs[jn][2 * h + 1]} + f32x2{sh[jn][2 * h], sh[jn][2 * h + 1]};
        a2[i][jn][h] = t2[i][jn][h] * kNL2E;
      }
#pragma unroll
  for (int i = 0; i < 5; ++i)
#pragma unroll
    for (int jn = 0; jn < 2; ++jn)
#pragma unroll
      for (int h = 0; h < 2; ++h) {
        a2[i][jn][h][0] = __builtin_amdgcn_exp2f(a2[i][jn][h][0]);
        a2[i][jn][h][1] = __builtin_amdgcn_exp2f(a2[i][jn][h][1]);
        a2[i][jn][h] = a2[i][jn][h] + 1.0f;
      }
#pragma unroll
  for (int i = 0; i < 5; ++i) {
    unsigned d[2][2];
#pragma unroll
    for (int jn = 0; jn < 2; ++jn)
#pragma unroll
      for (int h = 0; h < 2; ++h) {
        const f32x2 r = {__builtin_amdgcn_rcpf(a2[i][jn][h][0]), __builtin_amdgcn_rcpf(a2[i][jn][h][1])};
        d[jn][h] = pack_bf16x2(t2[i][jn][h] * r);
      }
    const auto s0v = __builtin_amdgcn_permlane16_swap(d[0][0], d[1][0], false, false);
    const auto s1v = __builtin_amdgcn_permlane16_swap(d[0][1], d[1][1], false, false);
    const u32x4 v = {s0v[0], s1v[0], s0v[1], s1v[1]};
    const unsigned off = 16 * i + prow < limit ? obase + (unsigned)(16 * i * kC * 2) : kOOB;
    __builtin_amdgcn_raw_buffer_store_b128(v, rout, off, 0, 0);
  }
}

extern "C" void kernel_launch(void* const* d_in, const int* in_sizes, int n_in,
                              void* d_out, int out_size, void* d_ws, size_t ws_size,
                              hipStream_t stream) {
  (void)in_sizes; (void)n_in; (void)out_size; (void)d_ws; (void)ws_size;
  const __hip_bfloat16* x = (const __hip_bfloat16*)d_in[0];
  const __hip_bfloat16* w = (const __hip_bfloat16*)d_in[1];
  const __hip_bfloat16* mean = (const __hip_bfloat16*)d_in[2];
  const __hip_bfloat16* var = (const __hip_bfloat16*)d_in[3];
  const __hip_bfloat16* g = (const __hip_bfloat16*)d_in[4];
  const __hip_bfloat16* b = (const __hip_bfloat16*)d_in[5];
  __hip_bfloat16* out = (__hip_bfloat16*)d_out;
  (void)hipFuncSetAttribute((const void*)conv3_bn_silu_kernel, hipFuncAttributeMaxDynamicSharedMemorySize, kLds);
  conv3_bn_silu_kernel<<<kTiles, 512, kLds, stream>>>(x, w, mean, var, g, b, out);
}
```
